```python
import torch

def _softmax(x, axis=-1):
    return torch.softmax(x, dim=axis)
def _gelu(x):
    return torch.nn.functional.gelu(x, approximate="tanh")
def _silu(x):
    return torch.nn.functional.silu(x)
def _sigmoid(x):
    return torch.sigmoid(x)
def _layernorm(x, axis=-1, eps=1e-5):
    m = x.mean(dim=axis, keepdim=True)
    v = x.var(dim=axis, keepdim=True, unbiased=False)
    return (x - m) / torch.sqrt(v + eps)
def _scatter_add(tgt, idx, src, axis=0):
    return tgt.clone().index_add_(axis, idx, src)
def _segment_sum(src, seg, n):
    out = torch.zeros((n,) + src.shape[1:], dtype=src.dtype)
    return out.index_add_(0, seg, src)
def _pad(x, pw):
    flat = tuple(v for pair in reversed(pw) for v in pair)
    return torch.nn.functional.pad(x, flat)

kernel_name = 'gated_mix__sliding_local_dilated__glu_gated_glu__prompt'

def setup_inputs():

    _w = (lambda a: (lambda t: t.to(torch.bfloat16) if t.is_floating_point() else t)(torch.as_tensor(a)))
    def _A_setup():
        B, S, D = 1, 2048, 512
        return {
            "x": _w((lambda *s: torch.randn(*s, dtype=torch.bfloat16) * 0.05)(B, S, D)),
            "wqkv": _w((lambda *s: torch.randn(*s, dtype=torch.bfloat16) * 0.8)(D, 3 * D)),
            "wo": _w((lambda *s: torch.randn(*s, dtype=torch.bfloat16) * 0.05)(D, D)),
        }
    _A_in = _A_setup()

    def _B_setup():
        B, S, D, F = 1, 2048, 512, 1792
        return {
            "x": _w((lambda *s: torch.randn(*s, dtype=torch.bfloat16) * 0.05)(B, S, D)),
            "w_gate": _w((lambda *s: torch.randn(*s, dtype=torch.bfloat16) * 3.2)(D, F)),
            "w_up": _w((lambda *s: torch.randn(*s, dtype=torch.bfloat16) * 0.1)(D, F)),
            "w_down": _w((lambda *s: torch.randn(*s, dtype=torch.bfloat16) * 0.1)(F, D)),
        }
    _B_in = _B_setup()

    return {
        "A_x": _A_in["x"],
        "A_wqkv": _A_in["wqkv"],
        "A_wo": _A_in["wo"],
        "B_x": _B_in["x"],
        "B_w_gate": _B_in["w_gate"],
        "B_w_up": _B_in["w_up"],
        "B_w_down": _B_in["w_down"],
        "gate_w": _w((lambda *s: torch.randn(*s, dtype=torch.bfloat16) * 0.05)(512) * 16.0),
    }

def reference(A_x, A_wqkv, A_wo, B_x, B_w_gate, B_w_up, B_w_down, gate_w):
    def _A_ref(x, wqkv, wo):
        B, S, D = x.shape
        H = 32; Dh = D // H
        W, dil = 8, 2
        qkv = (lambda x, *d: x.permute(*d))((x @ wqkv).reshape(B, S, 3, H, Dh), 2, 0, 3, 1, 4)
        q, k, v = qkv[0], qkv[1], qkv[2]
        scale = Dh ** -0.5
        pos = torch.arange(S)
        off = torch.arange(W) * dil
        idx = pos[:, None] - off[None, :]
        mask = idx >= 0
        idx_c = torch.where(mask, idx, 0).reshape(-1)
        kg = k[:, :, idx_c, :].reshape(B, H, S, W, Dh)
        vg = v[:, :, idx_c, :].reshape(B, H, S, W, Dh)
        logits = torch.einsum("bhsd,bhswd->bhsw", q, kg) * scale
        logits = torch.where(mask[None, None, :, :], logits, -1e9)
        attn = _softmax(logits, axis=-1)
        o = torch.einsum("bhsw,bhswd->bhsd", attn, vg)
        return (lambda x, *d: x.permute(*d))(o, 0, 2, 1, 3).reshape(B, S, D) @ wo

    def _B_ref(x, w_gate, w_up, w_down):
        g = torch.einsum("bsd,df->bsf", x, w_gate)
        u = torch.einsum("bsd,df->bsf", x, w_up)
        h = _sigmoid(g) * u
        return torch.einsum("bsf,fd->bsd", h, w_down)

    x = A_x
    g = _sigmoid((x * gate_w).sum(-1, keepdims=True))
    a = _A_ref(x, A_wqkv, A_wo)
    b = _B_ref(x, B_w_gate, B_w_up, B_w_down)
    return g * a + (1 - g) * b
```

```cpp
#include <hip/hip_runtime.h>
#include <hip/hip_bf16.h>
#include <stdint.h>
#include <utility>
#pragma clang diagnostic ignored "-Winline-asm"

typedef short s16x4 __attribute__((ext_vector_type(4)));
typedef __bf16 bf16x2 __attribute__((ext_vector_type(2)));
typedef __bf16 bf16x8 __attribute__((ext_vector_type(8)));
typedef float f32x2 __attribute__((ext_vector_type(2)));
typedef float f32x4 __attribute__((ext_vector_type(4)));
typedef unsigned u32x2 __attribute__((ext_vector_type(2)));
typedef unsigned u32x4 __attribute__((ext_vector_type(4)));
typedef __attribute__((address_space(3))) s16x4* lds_s16x4_p;
typedef __attribute__((address_space(3))) u32x4* lds_u32x4_p;
typedef __attribute__((address_space(3))) u32x2* lds_u32x2_p;
typedef __attribute__((address_space(3))) f32x4* lds_f32x4_p;
typedef __attribute__((address_space(1))) unsigned long long* g_u64_p;
typedef __amdgpu_buffer_rsrc_t rsrc_t;

#define DI __device__ __forceinline__

constexpr int S_ = 2048, D_ = 512, F_ = 1792;
constexpr int KS_ = 72;
constexpr unsigned H_BYTES = 128u * KS_ * 1024u;
constexpr unsigned G_OFF = H_BYTES;
constexpr unsigned HG_BYTES = G_OFF + S_ * 4;
constexpr unsigned FLAG_OFF = HG_BYTES;
constexpr unsigned EPOCH_OFF = FLAG_OFF + 256 * 128;
constexpr unsigned OOB = 0x7ffffff0u;
constexpr int LDS_BYTES = 163840;
constexpr int PF_LDS = 163584;
constexpr int XCHG_LDS = 151552;

template <int N> using ic = std::integral_constant<int, N>;
template <class Fn, int... I> DI void sfor_impl(Fn&& f, std::integer_sequence<int, I...>) { (f(ic<I>{}), ...); }
template <int N, class Fn> DI void sfor(Fn&& f) { sfor_impl(f, std::make_integer_sequence<int, N>{}); }

template <int N> DI void wait_vm() { asm volatile("s_waitcnt vmcnt(%0)" ::"i"(N) : "memory"); }
DI void wait_lgkm0() { asm volatile("" ::: "memory"); __builtin_amdgcn_s_waitcnt(0xC07F); asm volatile("" ::: "memory"); }
DI void bar() { asm volatile("" ::: "memory"); __builtin_amdgcn_s_barrier(); asm volatile("" ::: "memory"); }
DI void sched0() { __builtin_amdgcn_sched_barrier(0); }
DI int rfl(int x) { return __builtin_amdgcn_readfirstlane(x); }
template <class T> DI void pin(T& x) { asm volatile("" : "+v"(x)); }

DI rsrc_t mk_rsrc(const void* p, unsigned bytes) { return __builtin_amdgcn_make_buffer_rsrc((void*)p, 0, bytes, 0x00020000); }
DI void dma16(rsrc_t r, unsigned lds_off, unsigned voff, unsigned soff) {
    asm volatile("s_mov_b32 m0, %0\n\ts_nop 0\n\tbuffer_load_dwordx4 %1, %2, %3 offen lds"
                 ::"s"(lds_off), "v"(voff), "s"(r), "s"(soff) : "memory", "m0");
}
DI void dma_m0(unsigned lds_off) { asm volatile("s_mov_b32 m0, %0" ::"s"(lds_off) : "memory", "m0"); }
DI void dma_go(rsrc_t r, unsigned voff, unsigned soff) {
    asm volatile("buffer_load_dwordx4 %0, %1, %2 offen lds" ::"v"(voff), "s"(r), "s"(soff) : "memory");
}
DI void ld16_sc1(u32x4& d, rsrc_t r, unsigned voff, unsigned soff) {
    asm volatile("buffer_load_dwordx4 %0, %1, %2, %3 offen sc1" : "=v"(d) : "v"(voff), "s"(r), "s"(soff) : "memory");
}
DI void ld16(u32x4& d, rsrc_t r, unsigned voff, unsigned soff) {
    asm volatile("buffer_load_dwordx4 %0, %1, %2, %3 offen" : "=v"(d) : "v"(voff), "s"(r), "s"(soff) : "memory");
}
DI void ld4_sc1(unsigned& d, rsrc_t r, unsigned voff) {
    asm volatile("buffer_load_dword %0, %1, %2, 0 offen sc1" : "=v"(d) : "v"(voff), "s"(r) : "memory");
}
DI void ld8_sc1(u32x2& d, const void* p) { asm volatile("global_load_dwordx2 %0, %1, off sc1" : "=v"(d) : "v"(p) : "memory"); }
DI void st16_sc1(u32x4 v, rsrc_t r, unsigned voff) { __builtin_amdgcn_raw_buffer_store_b128(v, r, voff, 0, 16); }

DI bf16x8 lds_b128(unsigned off) {
    extern __shared__ __attribute__((aligned(16))) char smem[];
    return __builtin_bit_cast(bf16x8, *(lds_u32x4_p)(smem + off));
}
DI bf16x8 lds_tr2(unsigned off, unsigned delta) {
    extern __shared__ __attribute__((aligned(16))) char smem[];
    s16x4 a = __builtin_amdgcn_ds_read_tr16_b64_v4i16((lds_s16x4_p)(smem + off));
    s16x4 b = __builtin_amdgcn_ds_read_tr16_b64_v4i16((lds_s16x4_p)(smem + off + delta));
    return __builtin_bit_cast(bf16x8, __builtin_shufflevector(a, b, 0, 1, 2, 3, 4, 5, 6, 7));
}
DI s16x4 lds_tr(unsigned off) {
    extern __shared__ __attribute__((aligned(16))) char smem[];
    return __builtin_amdgcn_ds_read_tr16_b64_v4i16((lds_s16x4_p)(smem + off));
}
DI bf16x8 join(s16x4 a, s16x4 b) { return __builtin_bit_cast(bf16x8, __builtin_shufflevector(a, b, 0, 1, 2, 3, 4, 5, 6, 7)); }
constexpr int read_gap(int r, int R, int G) { return r * (G - 3) / R; }
DI f32x4 mfma32(bf16x8 a, bf16x8 b, f32x4 c) { return __builtin_amdgcn_mfma_f32_16x16x32_bf16(a, b, c, 0, 0, 0); }
DI f32x4 mfma16(u32x2 a, u32x2 b, f32x4 c) {
    return __builtin_amdgcn_mfma_f32_16x16x16bf16_1k(__builtin_bit_cast(s16x4, a), __builtin_bit_cast(s16x4, b), c, 0, 0, 0);
}
DI unsigned pk_bf16(float lo, float hi) { return __builtin_bit_cast(unsigned, __builtin_convertvector((f32x2){lo, hi}, bf16x2)); }
DI u32x2 pk4(f32x4 v) { return (u32x2){pk_bf16(v[0], v[1]), pk_bf16(v[2], v[3])}; }
DI float bf_lo(unsigned u) { return __builtin_bit_cast(float, u << 16); }
DI float bf_round(float x) { return bf_lo(pk_bf16(x, x)); }
DI float sigmoidf_(float x) { return __builtin_amdgcn_rcpf(1.0f + __builtin_amdgcn_exp2f(x * -1.44269504089f)); }
DI float as_f32(unsigned u) { return __builtin_bit_cast(float, u); }
DI unsigned as_u32(float f) { return __builtin_bit_cast(unsigned, f); }
DI float max2(float a, float b) { return __builtin_elementwise_maximum(a, b); }
template <int CTRL> DI float dpp_add(float v) {
    return v + as_f32(__builtin_amdgcn_update_dpp(0, as_u32(v), CTRL, 0xf, 0xf, true));
}
DI float rows_sum(float m) {
    const auto a = __builtin_amdgcn_permlane32_swap(as_u32(m), as_u32(m), false, false);
    const unsigned a0 = a[0], a1 = a[1];
    m = as_f32(a0) + as_f32(a1);
    const auto b = __builtin_amdgcn_permlane16_swap(as_u32(m), as_u32(m), false, false);
    const unsigned b0 = b[0], b1 = b[1];
    return as_f32(b0) + as_f32(b1);
}
DI float wave_sum(float v) { return rows_sum(dpp_add<0x140>(dpp_add<0x141>(dpp_add<0x4E>(dpp_add<0xB1>(v))))); }

DI int mlp_first_frag(int rb) { return rb < 8 ? 11 * rb : 88 + 10 * (rb - 8); }
DI int mlp_block_of(int tf) { return tf < 88 ? tf / 11 : 8 + (tf - 88) / 10; }
DI int frag_token(int tf, int i) { return 32 * (tf >> 1) + 2 * i + (tf & 1); }

#define STEP_SYNC(N) do { wait_lgkm0(); wait_vm<N>(); bar(); sched0(); } while (0)

struct Args {
    const void* x; const void* wqkv; const void* wo; const void* wg; const void* wu; const void* wd; const void* gw;
    char* ws; void* out;
};

constexpr int w2_page(int j) { constexpr int base[9] = {33, 0, 4, 17, 8, 12, 21, 25, 29}; return base[j % 9]; }
struct W2Loader {
    rsrc_t ra, rd;
    unsigned soff_a, soff_d, v[2], lds, ra_[4];
    DI void init(const Args& a, int w, int lane, int ct) {
        const int kq = w & 3, th = w >> 2;
        rd = mk_rsrc(a.wd, F_ * D_ * 2);
        ra = mk_rsrc(kq == 0 ? a.wo : a.wd, kq == 0 ? D_ * D_ * 2 : F_ * D_ * 2);
        soff_d = (unsigned)((18 * kq - 16) * 32768);
        soff_a = kq == 0 ? 0u : soff_d;
#pragma unroll
        for (int m = 0; m < 2; ++m) {
            const int row = 8 * (2 * th + m) + (lane >> 3);
            v[m] = row * 1024 + ct * 128 + (((lane & 7) ^ (((row >> 1) & 3) << 1)) << 4);
        }
        lds = kq * 4096 + 2 * th * 1024;
        const int q = lane >> 4, qq = (lane & 15) >> 2;
#pragma unroll
        for (int nf = 0; nf < 4; ++nf) ra_[nf] = kq * 4096 + (4 * q + qq) * 128 + ((nf ^ ((2 * q + (qq >> 1)) & 3)) << 5) + 8 * (lane & 3);
    }
    DI bf16x8 frag(int j, int nf) const { return lds_tr2(ra_[nf] + w2_page(j) * 4096, 2048); }
    template <int N, int PH = 2> DI void piece() const {
        constexpr int j = N >> 1, m = N & 1;
        const unsigned dst = lds + (w2_page(j) * 4096 + m * 1024), so = (j < 16 ? soff_a : soff_d) + j * 32768;
        if constexpr (PH == 2) dma16(j < 16 ? ra : rd, dst, v[m], so);
        else if constexpr (PH == 0) dma_m0(dst);
        else dma_go(j < 16 ? ra : rd, v[m], so);
    }
};

DI void touch_line(rsrc_t r, unsigned voff) {
    asm volatile("s_mov_b32 m0, %0\n\ts_nop 4\n\tbuffer_load_dword %1, %2, 0 offen lds" ::"s"(PF_LDS), "v"(voff), "s"(r) : "memory", "m0");
}
DI void touch_w2(const Args& a, int gw, int lane) {
    touch_line(mk_rsrc(a.wd, F_ * D_ * 2), lane < 7 ? (unsigned)((7 * gw + lane) * 128) : OOB);
    touch_line(mk_rsrc(a.wo, D_ * D_ * 2), lane < 2 ? (unsigned)((2 * gw + lane) * 128) : OOB);
}

DI void touch_handoff(const Args& a, int t, int gentry, int lane) {
    touch_line(mk_rsrc(a.ws, EPOCH_OFF), lane == 0 ? FLAG_OFF + t * 128 : (lane == 1 ? G_OFF + gentry * 4 : OOB));
}
DI void touch_slow_column(const Args& a, int g, int lane) {
    const unsigned cd = (3u - ((unsigned)(uint64_t)a.wd >> 7)) & 7u, co = (3u - ((unsigned)(uint64_t)a.wo >> 7)) & 7u;
    touch_line(mk_rsrc(a.wd, F_ * D_ * 2), lane < 7 ? (unsigned)((7 * g + lane) * 1024 + cd * 128) : OOB);
    touch_line(mk_rsrc(a.wo, D_ * D_ * 2), lane < 2 ? (unsigned)((2 * g + lane) * 1024 + co * 128) : OOB);
}

DI void touch_code(int idx, int w, int lane) {
    uint64_t pc; unsigned len;
    asm volatile("s_getpc_b64 %0\ngm_code_begin:\n\ts_mov_b32 %1, gm_code_end-gm_code_begin" : "=s"(pc), "=s"(len));
    touch_line(mk_rsrc((const void*)pc, len), lane < 2 ? (unsigned)(((idx * 8 + w) * 2 + lane) * 128) : OOB);
}

struct Gate {
    u32x4 x, w;
    float s;
    DI void load(const Args& a, int entry, int lane) {
        ld16(x, mk_rsrc(a.x, S_ * D_ * 2), lane * 16, frag_token(entry >> 4, entry & 15) * 1024);
        ld16(w, mk_rsrc(a.gw, D_ * 2), lane * 16, 0);
    }
    DI void part_a() {
        s = 0.f;
#pragma unroll
        for (int c = 0; c < 4; ++c) {
            const unsigned xc = x[c], wc = w[c];
            s = __builtin_amdgcn_fdot2_f32_bf16(__builtin_bit_cast(bf16x2, xc), __builtin_bit_cast(bf16x2, wc), s, false);
        }
        pin(s);
    }
    DI void part_b() { s = bf_round(sigmoidf_(bf_round(wave_sum(s)))); pin(s); }
    DI void store(rsrc_t rh, int entry, int lane) const {
        __builtin_amdgcn_raw_buffer_store_b32(as_u32(s), rh, lane == 0 ? (unsigned)(G_OFF + entry * 4) : OOB, 0, 16);
    }
};

template <int NF>
DI void mlp_wave(const Args& a, W2Loader& w2, bf16x8 (&wr)[9][4], const int ct, const int idx, const int gentry, const int w, const int lane, const int rb, const int cg, const int F0, u32x2& ein) {
    constexpr int WSLOT = 16384, XBASE = 65536, XSLOT = 28672, WSTEP = 32 * F_ * 2;
    const int f0 = mlp_first_frag(rb), nfr = rb < 8 ? 11 : 10;
    const rsrc_t rx = mk_rsrc(a.x, S_ * D_ * 2), rg = mk_rsrc(a.wg, D_ * F_ * 2), ru = mk_rsrc(a.wu, D_ * F_ * 2);
    Gate gate;
    const int wc = w & 3;
    const int q = lane >> 4, i16 = lane & 15, qq = i16 >> 2, p = lane & 3;

    const unsigned vw = (4 * w + q) * (F_ * 2) + cg * 256 + ((i16 ^ ((q | ((2 * w) & 4)) << 1)) << 4);
    const int r16 = 8 * (w & 1) + (lane >> 3);
    const unsigned vxl = r16 * 2048 + (((lane & 7) ^ (r16 >> 1)) << 4), vx2 = 16 + w < 2 * nfr ? vxl : OOB;
    unsigned sx[3];
#pragma unroll
    for (int j = 0; j < 3; ++j) { const int tf = f0 + 4 * j + (w >> 1); sx[j] = (32 * (tf >> 1) + (tf & 1)) * 1024; }
    const unsigned wl = w * 1024;
    auto dmap = [&](auto PHI, auto P, auto ODD, unsigned wdst, unsigned wsoff, unsigned xdst, unsigned xsoff) {
        constexpr int pc = decltype(P)::value, PH = decltype(PHI)::value;
        constexpr bool live = pc < 3 || decltype(ODD)::value;
        if constexpr (live) {
            constexpr int xi = decltype(ODD)::value ? pc - 2 : 2;
            const unsigned dst = pc < 2 ? wdst + wl + pc * 8192 : xdst + wl + xi * 8192;
            const rsrc_t r = pc == 0 ? rg : (pc == 1 ? ru : rx);
            const unsigned v = pc < 2 ? vw : (xi == 2 ? vx2 : vxl), so = pc < 2 ? wsoff : xsoff + sx[pc < 2 ? 0 : xi];
            if constexpr (PH == 2) dma16(r, dst, v, so);
            else if constexpr (PH == 0) dma_m0(dst);
            else dma_go(r, v, so);
        }
    };
    auto dma = [&](auto P, auto ODD, unsigned wdst, unsigned wsoff, unsigned xdst, unsigned xsoff, unsigned) {
        dmap(ic<2>{}, P, ODD, wdst, wsoff, xdst, xsoff);
    };
    unsigned xa_[2], wa_[2];
#pragma unroll
    for (int kk = 0; kk < 2; ++kk) xa_[kk] = XBASE + F0 * 2048 + i16 * 128 + (((4 * kk + q) ^ (i16 >> 1)) << 4);
#pragma unroll
    for (int e = 0; e < 2; ++e) wa_[e] = (8 * q + qq) * 256 + (((2 * wc + e) ^ (qq | ((q & 1) << 2))) << 5) + 8 * p;

    f32x4 ag[NF][2], au[NF][2];
#pragma unroll
    for (int f = 0; f < NF; ++f)
#pragma unroll
        for (int e = 0; e < 2; ++e) { ag[f][e] = (f32x4){0, 0, 0, 0}; au[f][e] = (f32x4){0, 0, 0, 0}; }
    bf16x8 xf[2][NF];
    s16x4 wh[2][4][2];

    auto rd = [&](auto K, auto B, unsigned rw, unsigned rxo) {
        constexpr int k = decltype(K)::value, b = decltype(B)::value;
        if constexpr (k < 8) wh[b][k >> 1][k & 1] = lds_tr(wa_[k >> 2] + rw + ((k >> 1) & 1) * 8192 + (k & 1) * 1024);
        else xf[b][k - 8] = lds_b128(xa_[b] + rxo + (k - 8) * 2048);
    };
    auto step = [&](auto PAR, auto RD, auto MODE, unsigned rw, unsigned rxo, auto&& side) {
        constexpr int bc = decltype(PAR)::value, bn = bc ^ 1, NR = 8 + NF, md = decltype(MODE)::value;
        constexpr int ND = md == 0 ? 4 : (md == 1 ? 8 : 7), DS = md == 0 ? (NF == 6 ? 6 : 5) : (md == 1 ? 2 : 3);
        constexpr int D0 = NF == 6 ? 0 : (md == 2 ? 1 : 2), G = 4 * NF - ND;
        sfor<4 * NF>([&](auto J) {
            constexpr int j = decltype(J)::value;
            constexpr int jj = (j % NF) * 4 + j / NF, f = jj >> 2, e = (jj >> 1) & 1;
            constexpr bool isd = j >= D0 && (j - D0) % DS == 0 && (j - D0) / DS < ND;
            constexpr int nd0 = j < D0 ? 0 : (j - D0) / DS + 1, nd = nd0 < ND ? nd0 : ND;
            if constexpr (isd) { side(ic<0>{}, ic<nd - 1>{}); sched0(); }
            if constexpr (jj & 1) au[f][e] = mfma32(join(wh[bc][2 * e + 1][0], wh[bc][2 * e + 1][1]), xf[bc][f], au[f][e]);
            else ag[f][e] = mfma32(join(wh[bc][2 * e][0], wh[bc][2 * e][1]), xf[bc][f], ag[f][e]);
            if constexpr (isd) { sched0(); side(ic<1>{}, ic<nd - 1>{}); }
            else if constexpr (decltype(RD)::value)
                sfor<NR>([&](auto R) { if constexpr (read_gap(decltype(R)::value, NR, G) == j - nd) rd(R, ic<bn>{}, rw, rxo); });
            sched0();
        });
    };

    dma(ic<2>{}, ic<1>{}, 0, 0, XBASE, 0, 0); dma(ic<3>{}, ic<1>{}, 0, 0, XBASE, 0, 0);
    sfor<3>([&](auto P) { dma(P, ic<0>{}, 0, 0, XBASE, 0, 0); });
    sfor<4>([&](auto P) { dma(P, ic<1>{}, WSLOT, WSTEP, XBASE + XSLOT, 128, 0); });
    u32x2 e_;
    ld8_sc1(e_, a.ws + EPOCH_OFF);
    gate.load(a, gentry, lane);
    sched0();
    w2.init(a, w, lane, ct);
    wait_vm<7>();
    bar();
    sfor<8 + NF>([&](auto K) { rd(K, ic<0>{}, 0, 0); });
    sfor<3>([&](auto P) { dma(P, ic<0>{}, 2 * WSLOT, 2 * WSTEP, XBASE + XSLOT, 128, 0); });
    sched0();
    STEP_SYNC(8);
    step(ic<0>{}, ic<1>{}, ic<2>{}, WSLOT, 0, [&](auto PH, auto K) {
        constexpr int k = decltype(K)::value;
        if constexpr (k < 4) dmap(PH, K, ic<1>{}, 3 * WSLOT, 3 * WSTEP, XBASE + 2 * XSLOT, 256);
        else dmap(PH, ic<k - 4>{}, ic<0>{}, 0, 4 * WSTEP, XBASE + 2 * XSLOT, 256);
    });
    STEP_SYNC(7);
    pin(e_); pin(gate.x); pin(gate.w);
    ein = (u32x2){(unsigned)rfl(e_[0]), (unsigned)rfl(e_[1])};
    step(ic<1>{}, ic<1>{}, ic<0>{}, 2 * WSLOT, XSLOT, [&](auto PH, auto K) {
        dmap(PH, K, ic<1>{}, WSLOT, 5 * WSTEP, XBASE, 384);
    });
    touch_w2(a, gentry, lane);
    touch_handoff(a, gentry >> 3, gentry, lane);
    sched0();

    unsigned wa = 2 * WSLOT, x0 = XSLOT, x1 = 2 * XSLOT, x2 = 0;
#pragma unroll 1
    for (int t = 1; t < 5; ++t) {
        const unsigned ws = (2 * t + 4) * WSTEP, xs = (t + 2) * 128;
        STEP_SYNC(7);
        step(ic<0>{}, ic<1>{}, ic<0>{}, wa + WSLOT, x0, [&](auto PH, auto K) {
            if constexpr (decltype(K)::value < 3) dmap(PH, K, ic<0>{}, wa, ws, XBASE + x2, xs);
        });
        STEP_SYNC(7);
        step(ic<1>{}, ic<1>{}, ic<0>{}, wa ^ (2 * WSLOT), x1, [&](auto PH, auto K) {
            if constexpr (decltype(K)::value < 4) dmap(PH, K, ic<1>{}, wa + WSLOT, ws + WSTEP, XBASE + x0, xs + 128);
        });
        wa ^= 2 * WSLOT;
        const unsigned xt = x0; x0 = x1; x1 = x2; x2 = xt;
    }
    STEP_SYNC(7);
    step(ic<0>{}, ic<1>{}, ic<0>{}, 3 * WSLOT, 2 * XSLOT, [&](auto PH, auto K) {
        if constexpr (decltype(K)::value < 3) dmap(PH, K, ic<0>{}, 2 * WSLOT, 14 * WSTEP, XBASE + XSLOT, 7 * 128);
        if constexpr (decltype(K)::value == 3 && decltype(PH)::value == 1) touch_slow_column(a, idx * 8 + w, lane);
    });
    STEP_SYNC(7 + 2);
    step(ic<1>{}, ic<1>{}, ic<0>{}, 0, 0, [&](auto PH, auto K) {
        if constexpr (decltype(K)::value < 2) dmap(PH, K, ic<1>{}, 3 * WSLOT, 15 * WSTEP, 0, 0);
    });
    STEP_SYNC(5 + 2);
    step(ic<0>{}, ic<1>{}, ic<0>{}, WSLOT, 0, [&](auto PH, auto K) {
        if constexpr (decltype(K)::value < 4) w2.piece<decltype(K)::value, decltype(PH)::value>();
    });
    STEP_SYNC(6);
    step(ic<1>{}, ic<1>{}, ic<0>{}, 2 * WSLOT, XSLOT, [&](auto PH, auto K) {
        if constexpr (decltype(K)::value < 4) w2.piece<4 + decltype(K)::value, decltype(PH)::value>();
    });
    STEP_SYNC(8);
    step(ic<0>{}, ic<1>{}, ic<0>{}, 3 * WSLOT, XSLOT, [&](auto PH, auto K) {
        constexpr int k = decltype(K)::value, ph = decltype(PH)::value;
        if constexpr (k < 2) w2.piece<8 + k, ph>();
        else if constexpr (k == 2 && ph == 1) gate.part_a();
        else if constexpr (k == 3 && ph == 1) gate.part_b();
    });
    wait_lgkm0(); wait_vm<2>(); bar(); sched0();
    step(ic<1>{}, ic<0>{}, ic<1>{}, 0, 0, [&](auto PH, auto K) {
        if constexpr (decltype(K)::value < 8) w2.piece<10 + decltype(K)::value, decltype(PH)::value>();
        if constexpr (decltype(K)::value < 8 && decltype(PH)::value == 1) {
            constexpr int k = decltype(K)::value;
            wr[k >> 1][2 * (k & 1)] = w2.frag(k >> 1, 2 * (k & 1));
            wr[k >> 1][2 * (k & 1) + 1] = w2.frag(k >> 1, 2 * (k & 1) + 1);
        }
    });
    wait_lgkm0(); bar(); sched0();
    const rsrc_t rh = mk_rsrc(a.ws, HG_BYTES);
    const unsigned hs = (16 + 4 * cg + wc) * 1024;
    constexpr int A1 = NF == 6 ? 0 : 1;
    sfor<NF>([&](auto Fi) {
        constexpr int f = decltype(Fi)::value;
        const int tf = f0 + F0 + f;
        constexpr float L2E = -1.44269504089f;
        unsigned o[4];
        sfor<2>([&](auto E) {
            constexpr int e = decltype(E)::value, n = 2 * f + e;
            f32x2 d2[2];
#pragma unroll
            for (int r = 0; r < 2; ++r) {
                const f32x2 g2 = (f32x2){ag[f][e][2 * r], ag[f][e][2 * r + 1]} * L2E;
                d2[r] = (f32x2){__builtin_amdgcn_exp2f(g2[0]), __builtin_amdgcn_exp2f(g2[1])} + 1.0f;
            }
            if constexpr (n >= A1 && n < A1 + 8) { sched0(); w2.piece<18 + n - A1>(); sched0(); }
#pragma unroll
            for (int r = 0; r < 2; ++r) {
                const f32x2 h = (f32x2){au[f][e][2 * r], au[f][e][2 * r + 1]} *
                                (f32x2){__builtin_amdgcn_rcpf(d2[r][0]), __builtin_amdgcn_rcpf(d2[r][1])};
                o[2 * e + r] = pk_bf16(h[0], h[1]);
            }
        });
        st16_sc1((u32x4){o[0], o[1], o[2], o[3]}, rh, lane * 16 + tf * (KS_ * 1024) + hs);
    });
    gate.store(rh, gentry, lane);
    wait_vm<8 + NF + 1>();
}

template <int TH>
DI void qkv_wave(const Args& a, W2Loader& w2, bf16x8 (&wr)[9][4], const int ct, const int idx, const int gentry, const int w, const int lane, const int rb, const int hg, u32x2& ein) {
    extern __shared__ __attribute__((aligned(16))) char smem[];
    constexpr int WSLOT = 16384, XBASE = 65536, XSLOT = 28672, WSTEP = 32 * 3072;
    const int g0 = min(6 * rb, 58);
    const rsrc_t rx = mk_rsrc(a.x, S_ * D_ * 2), rw = mk_rsrc(a.wqkv, D_ * 3 * D_ * 2);
    Gate gate;
    const int hh = w & 3;
    constexpr int th = TH;
    const int q = lane >> 4, i16 = lane & 15, qq = i16 >> 2, p = lane & 3;

    constexpr int NW = TH ? 1 : 2, NXE = TH ? 2 : 1, CE = NW + NXE, CO = NW + 2;
    const int l8 = lane >> 3, r16 = 8 * (w & 1) + l8;
    const unsigned vw = (8 * (w & 3) + l8) * 3072 + hg * 128 + (((lane & 7) ^ ((((l8 >> 1) & 1) | ((2 * w + (l8 >> 2)) & 2)) << 1)) << 4);
    const unsigned sw0 = (w >> 2) * 1024;
    const unsigned vxl = r16 * 2048 + (((lane & 7) ^ (r16 >> 1)) << 4);
    const unsigned vx0 = vxl - (w == 1 ? 15360u : 0u), vx3 = w < 6 ? vxl : OOB;
    unsigned sx[4];
#pragma unroll
    for (int j = 0; j < 4; ++j) {
        const int n = j < 3 ? 8 * j + w : 20 + w, tf = min(2 * g0 + (n >> 1) - 1, 127);
        sx[j] = (j == 0 && w < 2) ? max(32 * g0 - 16, 0) * 1024 : (32 * (tf >> 1) + (tf & 1)) * 1024;
    }
    const unsigned wl = w * 1024, xl3 = min(20 + w, 26) * 1024;
    auto dmap = [&](auto PHI, auto P, auto ODD, unsigned wdst, unsigned wsoff, unsigned xdst, unsigned xsoff) {
        constexpr int pc = decltype(P)::value, PH = decltype(PHI)::value, odd = decltype(ODD)::value;
        if constexpr (pc < (odd ? CO : CE)) {
            constexpr int xp = pc < NW ? 0 : pc - NW, xi = odd ? xp : 2 + xp;
            const unsigned dst = pc < NW ? wdst + wl + pc * 8192 : (xi == 3 ? xdst + xl3 : xdst + wl + xi * 8192);
            const unsigned v = pc < NW ? vw : (xi == 0 ? vx0 : (xi == 3 ? vx3 : vxl));
            const unsigned so = pc < NW ? wsoff + (pc == 0 ? sw0 : 2048u) : xsoff + sx[xi];
            if constexpr (PH == 2) dma16(pc < NW ? rw : rx, dst, v, so);
            else if constexpr (PH == 0) dma_m0(dst);
            else dma_go(pc < NW ? rw : rx, v, so);
        }
    };
    auto dma_w = [&](unsigned wdst, unsigned wsoff) { sfor<NW>([&](auto P) { dmap(ic<2>{}, P, ic<1>{}, wdst, wsoff, 0, 0); }); };
    auto dma_x = [&](auto ODD, unsigned xdst, unsigned xsoff) {
        sfor<decltype(ODD)::value ? 2 : NXE>([&](auto P) { dmap(ic<2>{}, ic<NW + decltype(P)::value>{}, ODD, 0, 0, xdst, xsoff); });
    };
    unsigned xa_[2], xh_[2], wa_;
#pragma unroll
    for (int kk = 0; kk < 2; ++kk) {
        xa_[kk] = XBASE + (1 + 6 * th) * 2048 + i16 * 128 + (((4 * kk + q) ^ (i16 >> 1)) << 4);
        const int hr = th ? (16 * (5 + (i16 >> 3)) + 8 + (i16 & 7)) : i16;
        xh_[kk] = XBASE + hr * 128 + (((4 * kk + q) ^ ((hr >> 1) & 7)) << 4);
    }
    wa_ = (8 * q + qq) * 128 + ((hh ^ (((qq >> 1) & 1) | ((q & 1) << 1))) << 5) + 8 * p;

    f32x4 aq[6], ak[7], av[7];
#pragma unroll
    for (int f = 0; f < 7; ++f) { ak[f] = (f32x4){0, 0, 0, 0}; av[f] = (f32x4){0, 0, 0, 0}; if (f < 6) aq[f] = (f32x4){0, 0, 0, 0}; }
    bf16x8 xf[2][7];
    s16x4 wh[2][3][2];

    auto rd = [&](auto K, auto B, unsigned rwo, unsigned rxo) {
        constexpr int k = decltype(K)::value, b = decltype(B)::value;
        if constexpr (k < 6) wh[b][k >> 1][k & 1] = lds_tr(wa_ + rwo + (k >> 1) * 4096 + (k & 1) * 512);
        else if constexpr (k == 6) xf[b][0] = lds_b128(xh_[b] + rxo);
        else xf[b][k - 6] = lds_b128(xa_[b] + rxo + (k - 7) * 2048);
    };
    auto step = [&](auto PAR, auto RD, auto MODE, unsigned rwo, unsigned rxo, auto&& side) {
        constexpr int bc = decltype(PAR)::value, bn = bc ^ 1, md = decltype(MODE)::value;
        constexpr int ND = md == 0 ? 4 : (md == 1 ? 8 : 7), DS = md == 0 ? 5 : (md == 1 ? 2 : 3);
        constexpr int D0 = TH ? (md == 2 ? 1 : 2) : 0, G = 20 - ND;
        sfor<20>([&](auto J) {
            constexpr int j = decltype(J)::value;
            constexpr bool isd = j >= D0 && (j - D0) % DS == 0 && (j - D0) / DS < ND;
            constexpr int nd0 = j < D0 ? 0 : (j - D0) / DS + 1, nd = nd0 < ND ? nd0 : ND;
            if constexpr (isd) { side(ic<0>{}, ic<nd - 1>{}); sched0(); }
            const bf16x8 wq = join(wh[bc][0][0], wh[bc][0][1]), wk = join(wh[bc][1][0], wh[bc][1][1]), wv = join(wh[bc][2][0], wh[bc][2][1]);
            if constexpr (j < 6) aq[j] = mfma32(wq, xf[bc][j + 1], aq[j]);
            else if constexpr (j < 13) ak[j - 6] = mfma32(wk, xf[bc][j - 6], ak[j - 6]);
            else av[j - 13] = mfma32(xf[bc][j - 13], wv, av[j - 13]);
            if constexpr (isd) { sched0(); side(ic<1>{}, ic<nd - 1>{}); }
            else if constexpr (decltype(RD)::value)
                sfor<13>([&](auto R) { if constexpr (read_gap(decltype(R)::value, 13, G) == j - nd) rd(R, ic<bn>{}, rwo, rxo); });
            sched0();
        });
    };

    dma_x(ic<1>{}, XBASE, 0); dma_w(0, 0); dma_x(ic<0>{}, XBASE, 0);
    dma_w(WSLOT, WSTEP); dma_x(ic<1>{}, XBASE + XSLOT, 128);
    u32x2 e_;
    ld8_sc1(e_, a.ws + EPOCH_OFF);
    gate.load(a, gentry, lane);
    sched0();
    w2.init(a, w, lane, ct);
    constexpr float NEG = -1e30f, CEXP = 0.25f * 1.44269504089f;
    f32x4 m_cur, m_prev, m_h0, m_h1;
    {
        const bool has_halo = (th != 0) || (g0 != 0);
#pragma unroll
        for (int r = 0; r < 4; ++r) {
            const int d = i16 - (4 * q + r);
            m_cur[r] = (unsigned)d <= 7u ? 0.f : NEG;
            m_prev[r] = d <= -9 ? 0.f : NEG;
            m_h0[r] = (has_halo && d <= -1 && q < 2) ? 0.f : NEG;
            m_h1[r] = has_halo ? m_prev[r] : NEG;
        }
        pin(m_cur); pin(m_prev); pin(m_h0); pin(m_h1);
    }
    wait_vm<CO + 3>();
    bar();
    sfor<13>([&](auto K) { rd(K, ic<0>{}, 0, 0); });
    dma_w(2 * WSLOT, 2 * WSTEP); dma_x(ic<0>{}, XBASE + XSLOT, 128);
    sched0();
    STEP_SYNC(2 + 3 + CE);
    step(ic<0>{}, ic<1>{}, ic<2>{}, WSLOT, 0, [&](auto PH, auto K) {
        constexpr int k = decltype(K)::value;
        if constexpr (k < CO) dmap(PH, K, ic<1>{}, 3 * WSLOT, 3 * WSTEP, XBASE + 2 * XSLOT, 256);
        else dmap(PH, ic<k - CO>{}, ic<0>{}, 0, 4 * WSTEP, XBASE + 2 * XSLOT, 256);
    });
    STEP_SYNC(CO + CE);
    pin(e_); pin(gate.x); pin(gate.w);
    ein = (u32x2){(unsigned)rfl(e_[0]), (unsigned)rfl(e_[1])};
    step(ic<1>{}, ic<1>{}, ic<0>{}, 2 * WSLOT, XSLOT, [&](auto PH, auto K) {
        dmap(PH, K, ic<1>{}, WSLOT, 5 * WSTEP, XBASE, 384);
    });
    touch_w2(a, gentry, lane);
    touch_handoff(a, gentry >> 3, gentry, lane);
    sched0();

    unsigned wa = 2 * WSLOT, x0 = XSLOT, x1 = 2 * XSLOT, x2 = 0;
#pragma unroll 1
    for (int t = 1; t < 5; ++t) {
        const unsigned ws = (2 * t + 4) * WSTEP, xs = (t + 2) * 128;
        STEP_SYNC(CE + CO);
        step(ic<0>{}, ic<1>{}, ic<0>{}, wa + WSLOT, x0, [&](auto PH, auto K) {
            dmap(PH, K, ic<0>{}, wa, ws, XBASE + x2, xs);
        });
        STEP_SYNC(CE + CO);
        step(ic<1>{}, ic<1>{}, ic<0>{}, wa ^ (2 * WSLOT), x1, [&](auto PH, auto K) {
            dmap(PH, K, ic<1>{}, wa + WSLOT, ws + WSTEP, XBASE + x0, xs + 128);
        });
        wa ^= 2 * WSLOT;
        const unsigned xt = x0; x0 = x1; x1 = x2; x2 = xt;
    }
    STEP_SYNC(CE + CO);
    step(ic<0>{}, ic<1>{}, ic<0>{}, 3 * WSLOT, 2 * XSLOT, [&](auto PH, auto K) {
        dmap(PH, K, ic<0>{}, 2 * WSLOT, 14 * WSTEP, XBASE + XSLOT, 7 * 128);
        if constexpr (decltype(K)::value == 3 && decltype(PH)::value == 1) touch_slow_column(a, idx * 8 + w, lane);
    });
    STEP_SYNC(CO + CE + 2);
    step(ic<1>{}, ic<1>{}, ic<0>{}, 0, 0, [&](auto PH, auto K) {
        if constexpr (decltype(K)::value < NW) dmap(PH, K, ic<1>{}, 3 * WSLOT, 15 * WSTEP, 0, 0);
    });
    STEP_SYNC(CE + NW + 2);
    step(ic<0>{}, ic<1>{}, ic<0>{}, WSLOT, 0, [&](auto PH, auto K) {
        if constexpr (decltype(K)::value < 4) w2.piece<decltype(K)::value, decltype(PH)::value>();
    });
    STEP_SYNC(NW + 4);
    step(ic<1>{}, ic<1>{}, ic<0>{}, 2 * WSLOT, XSLOT, [&](auto PH, auto K) {
        if constexpr (decltype(K)::value < 4) w2.piece<4 + decltype(K)::value, decltype(PH)::value>();
    });
    STEP_SYNC(8);
    step(ic<0>{}, ic<1>{}, ic<0>{}, 3 * WSLOT, XSLOT, [&](auto PH, auto K) {
        constexpr int k = decltype(K)::value, ph = decltype(PH)::value;
        if constexpr (k < 2) w2.piece<8 + k, ph>();
        else if constexpr (k == 2 && ph == 1) gate.part_a();
        else if constexpr (k == 3 && ph == 1) gate.part_b();
    });
    wait_lgkm0(); wait_vm<2>(); bar(); sched0();
    step(ic<1>{}, ic<0>{}, ic<1>{}, 0, 0, [&](auto PH, auto K) {
        if constexpr (decltype(K)::value < 8) w2.piece<10 + decltype(K)::value, decltype(PH)::value>();
        if constexpr (decltype(K)::value < 8 && decltype(PH)::value == 1) {
            constexpr int k = decltype(K)::value;
            wr[k >> 1][2 * (k & 1)] = w2.frag(k >> 1, 2 * (k & 1));
            wr[k >> 1][2 * (k & 1) + 1] = w2.frag(k >> 1, 2 * (k & 1) + 1);
        }
    });
    wait_lgkm0(); bar(); sched0();

    u32x2 kb[7], vb[7], qb[6];
    f32x4 s0[6], s1[6];
#pragma unroll
    for (int f = 0; f < 7; ++f) kb[f] = pk4(ak[f]);
#pragma unroll
    for (int f = 0; f < 6; ++f) qb[f] = pk4(aq[f]);
    w2.piece<18>();
    sched0();
#pragma unroll
    for (int f = 0; f < 6; ++f) {
        s0[f] = mfma16(kb[f < 2 ? 0 : f - 1], qb[f], f == 0 ? m_h0 : (f == 1 ? m_h1 : m_prev));
        s1[f] = mfma16(kb[f + 1], qb[f], m_cur);
        if (f < 4) vb[2 * f < 7 ? 2 * f : 6] = pk4(av[2 * f < 7 ? 2 * f : 6]);
        if (2 * f + 1 < 7) vb[2 * f + 1] = pk4(av[2 * f + 1]);
    }
    w2.piece<19>();
    sched0();
    const bool sel1 = q >= 2 && i16 < 8;
    bool sel0[4];
#pragma unroll
    for (int r = 0; r < 4; ++r) sel0[r] = q < 2 && 4 * q + r > i16;
    const unsigned M1 = sel1 ? 0xffffffffu : 0u;
    const unsigned M0[2] = {(sel0[0] ? 0xffffu : 0u) | (sel0[1] ? 0xffff0000u : 0u), (sel0[2] ? 0xffffu : 0u) | (sel0[3] ? 0xffff0000u : 0u)};
    f32x4 tv[6];
    float mx[6];
#pragma unroll
    for (int f = 0; f < 6; ++f)
#pragma unroll
        for (int r = 0; r < 4; ++r) tv[f][r] = (f == 0 ? sel0[r] : sel1) ? s0[f][r] : s1[f][r];
#pragma unroll
    for (int f = 0; f < 6; ++f) mx[f] = max2(max2(tv[f][0], tv[f][1]), tv[f][2]);
#pragma unroll
    for (int f = 0; f < 6; ++f) mx[f] = max2(mx[f], tv[f][3]);
    w2.piece<20>();
    sched0();
    {
        unsigned a0[6], a1[6];
#pragma unroll
        for (int f = 0; f < 6; ++f) { const auto r = __builtin_amdgcn_permlane32_swap(as_u32(mx[f]), as_u32(mx[f]), false, false); a0[f] = r[0]; a1[f] = r[1]; }
        sched0();
#pragma unroll
        for (int f = 0; f < 6; ++f) mx[f] = max2(as_f32(a0[f]), as_f32(a1[f]));
        w2.piece<21>();
        sched0();
#pragma unroll
        for (int f = 0; f < 6; ++f) { const auto r = __builtin_amdgcn_permlane16_swap(as_u32(mx[f]), as_u32(mx[f]), false, false); a0[f] = r[0]; a1[f] = r[1]; }
        sched0();
#pragma unroll
        for (int f = 0; f < 6; ++f) mx[f] = max2(as_f32(a0[f]), as_f32(a1[f])) * CEXP;
        sched0();
    }
    bf16x8 pfrag[6];
#pragma unroll
    for (int f = 0; f < 6; ++f)
#pragma unroll
        for (int r = 0; r < 4; ++r) tv[f][r] = __builtin_fmaf(tv[f][r], CEXP, -mx[f]);
    w2.piece<22>();
    sched0();
#pragma unroll
    for (int f = 0; f < 6; ++f)
#pragma unroll
        for (int r = 0; r < 4; ++r) tv[f][r] = __builtin_amdgcn_exp2f(tv[f][r]);
    w2.piece<24>();
    sched0();
#pragma unroll
    for (int f = 0; f < 6; ++f) {
        const u32x2 pp = pk4(tv[f]);
        const unsigned ma = f == 0 ? M0[0] : M1, mb = f == 0 ? M0[1] : M1;
        pfrag[f] = __builtin_bit_cast(bf16x8, (u32x4){pp[0] & ma, pp[1] & mb, pp[0] & ~ma, pp[1] & ~mb});
    }
    w2.piece<23>();
    sched0();
    const bf16x8 ones = __builtin_bit_cast(bf16x8, (u32x4){0x3f803f80u, 0x3f803f80u, 0x3f803f80u, 0x3f803f80u});
    f32x4 on[6], ln[6];
#pragma unroll
    for (int f = 0; f < 6; ++f) {
        const int pf = f < 2 ? 0 : f - 1;
        const bf16x8 vfrag = __builtin_bit_cast(bf16x8, (u32x4){vb[pf][0], vb[pf][1], vb[f + 1][0], vb[f + 1][1]});
        const f32x4 zero = {0, 0, 0, 0};
        ln[f] = mfma32(ones, pfrag[f], zero);
        on[f] = mfma32(vfrag, pfrag[f], zero);
    }
    sched0();
    float rl[6];
#pragma unroll
    for (int f = 0; f < 6; ++f) rl[f] = __builtin_amdgcn_rcpf(ln[f][0]);
    w2.piece<25>();
    sched0();
    u32x2 ob[6];
#pragma unroll
    for (int f = 0; f < 6; ++f) ob[f] = pk4(on[f] * rl[f]);
    sched0();
    const int odd = w & 1;
    u32x2 mine[3];
    if (odd) {
#pragma unroll
        for (int j = 0; j < 3; ++j) { *(lds_u32x2_p)(smem + XCHG_LDS + (w * 3 + j) * 512 + lane * 8) = ob[j]; mine[j] = ob[3 + j]; }
    } else {
#pragma unroll
        for (int j = 0; j < 3; ++j) { *(lds_u32x2_p)(smem + XCHG_LDS + (w * 3 + j) * 512 + lane * 8) = ob[3 + j]; mine[j] = ob[j]; }
    }
    wait_lgkm0();
    bar();
    const rsrc_t rh = mk_rsrc(a.ws, HG_BYTES);
    const unsigned hs = (2 * hg + (hh >> 1)) * 1024;
#pragma unroll
    for (int j = 0; j < 3; ++j) {
        const u32x2 other = *(lds_u32x2_p)(smem + XCHG_LDS + ((w ^ 1) * 3 + j) * 512 + lane * 8);
        const u32x4 o = odd ? (u32x4){other[0], other[1], mine[j][0], mine[j][1]} : (u32x4){mine[j][0], mine[j][1], other[0], other[1]};
        const int tf = 2 * (g0 + 3 * th) + 3 * odd + j;
        st16_sc1(o, rh, (tf >> 1) >= 6 * rb ? (unsigned)(lane * 16 + tf * (KS_ * 1024) + hs) : OOB);
    }
    gate.store(rh, gentry, lane);
    wait_vm<8 + 4>();
}

__global__ __launch_bounds__(512, 2) void fused_kernel(Args a) {
    extern __shared__ __attribute__((aligned(16))) char smem[];
    const int w = rfl(threadIdx.x >> 6), lane = threadIdx.x & 63;
    const int bid = blockIdx.x, xcd = bid & 7, idx = bid >> 3, t = xcd * 32 + idx;
    const int tt = 4 * xcd + (idx >> 3), ct = idx & 7;
    u32x2 ein;
    const int gentry = 8 * t + w;
    touch_code(idx, w, lane);
    W2Loader w2;
    bf16x8 wr[9][4];
    if (t < 88) {
        if (w < 4) qkv_wave<0>(a, w2, wr, ct, idx, gentry, w, lane, t >> 3, t & 7, ein);
        else qkv_wave<1>(a, w2, wr, ct, idx, gentry, w, lane, t >> 3, t & 7, ein);
    } else {
        const int m = t - 88, half = m / 84, r = m - half * 84, rb = r / 7, cg = half * 7 + (r - rb * 7);
        if (w < 4 && rb < 8) mlp_wave<6>(a, w2, wr, ct, idx, gentry, w, lane, rb, cg, 0, ein);
        else mlp_wave<5>(a, w2, wr, ct, idx, gentry, w, lane, rb, cg, w < 4 ? 0 : (rb < 8 ? 6 : 5), ein);
    }
    const unsigned long long tag = (((unsigned long long)ein[1] << 32) | ein[0]) + 1;

    bar();
    const int kq = w & 3, th = w >> 2;
    const int q = lane >> 4, i16 = lane & 15;
#pragma unroll
    for (int j = 4; j < 9; ++j)
#pragma unroll
        for (int nf = 0; nf < 4; ++nf) wr[j][nf] = w2.frag(j, nf);
    wait_lgkm0();
    wait_vm<0>();
    bar();
    char* const flags = a.ws + FLAG_OFF;
    if (threadIdx.x == 0) __hip_atomic_store((g_u64_p)(flags + t * 128), tag, __ATOMIC_RELAXED, __HIP_MEMORY_SCOPE_AGENT);
    if (w == 0) {
        int src;
        if (lane < 8) src = 8 * (tt / 3) + lane;
        else if (lane >= 36) src = 8 * tt + (lane & 7);
        else {
            const int l = lane - 8, hi = l >= 14, cg = min(l - 14 * hi, 13), rb = mlp_block_of(4 * tt + 3 * (hi ? 1 : 0));
            src = 88 + (cg >= 7 ? 84 + rb * 7 + cg - 7 : rb * 7 + cg);
        }
        const g_u64_p fp = (g_u64_p)(flags + src * 128);
        for (int spins = 0; spins < (1 << 16); ++spins) {
            const unsigned long long v = __hip_atomic_load(fp, __ATOMIC_RELAXED, __HIP_MEMORY_SCOPE_AGENT);
            if (__all(v == tag)) break;
            __builtin_amdgcn_s_sleep(1);
        }
    } else {
        sfor<10>([&](auto N) { w2.piece<26 + decltype(N)::value>(); });
        if (w < 6) {
            const rsrc_t ro = mk_rsrc(a.wo, D_ * D_ * 2);
            const int j = 12 + w;
            constexpr unsigned PAGES = 8u | (12u << 6) | (21u << 12) | (25u << 18) | (29u << 24);
            const unsigned page = (PAGES >> (6 * (w - 1))) & 63u;
#pragma unroll
            for (int m = 0; m < 2; ++m) {
                const unsigned v0 = (8 * m + (lane >> 3)) * 1024 + ct * 128 + (((lane & 7) ^ ((((8 * m + (lane >> 3)) >> 1) & 3) << 1)) << 4);
                if (j < 16) dma16(ro, page * 4096 + m * 1024, v0, j * 32768);
                else dma16(w2.rd, page * 4096 + m * 1024, v0, (j - 16) * 32768);
            }
        }
    }
    bar();

    constexpr int DEPTH = 5;
    const rsrc_t rh = mk_rsrc(a.ws, HG_BYTES);
    const unsigned hbase = ((4 * tt + 2 * th) * KS_ + 18 * kq) * 1024, hv = lane * 16;
    unsigned gv[2];
    u32x4 hf[18][2];
    auto ldh = [&](auto J) {
        constexpr int j = decltype(J)::value;
        if constexpr (j < 18) {
#pragma unroll
            for (int m = 0; m < 2; ++m) ld16_sc1(hf[j][m], rh, hv, hbase + (m * KS_ + j) * 1024);
        }
    };
    sfor<DEPTH>([&](auto J) {
        ldh(J);
        if constexpr ((decltype(J)::value & 1) == 1 && decltype(J)::value + 1 < DEPTH) bar();
    });
    f32x4 acc1[4][2], acc2[4][2];
#pragma unroll
    for (int nf = 0; nf < 4; ++nf)
#pragma unroll
        for (int m = 0; m < 2; ++m) { acc1[nf][m] = (f32x4){0, 0, 0, 0}; acc2[nf][m] = (f32x4){0, 0, 0, 0}; }
    bf16x8 wl[2][4];
    auto younger = [](int j, int now) constexpr {
        int n = 0;
        bool seen = false;
        for (int k = 0; k < DEPTH; ++k) { if (seen) n += 2; if (k == j) seen = true; }
        for (int s = 0; s < now; ++s) {
            const int k = DEPTH + s;
            if (k < 18) { if (seen) n += 2; if (k == j) seen = true; }
            if (seen && s == 2) n += 2;
        }
        return n;
    };
    sfor<18>([&](auto J) {
        constexpr int j = decltype(J)::value;
        constexpr int yn = younger(j, j);
        wait_vm<yn>();
        if constexpr (j == 8) bar();
        if constexpr (j == 14) { wait_lgkm0(); bar(); }
        pin(hf[j][0]); pin(hf[j][1]);
        if constexpr (j >= 8 && j < 17) {
#pragma unroll
            for (int nf = 0; nf < 4; ++nf) wl[(j + 1) & 1][nf] = w2.frag(j + 1, nf);
        }
#pragma unroll
        for (int nf = 0; nf < 4; ++nf)
#pragma unroll
            for (int m = 0; m < 2; ++m) {
                const bf16x8 wv = j < 9 ? wr[j < 9 ? j : 0][nf] : wl[j & 1][nf];
                const bf16x8 hv8 = __builtin_bit_cast(bf16x8, hf[j][m]);
                if constexpr (j < 16) acc1[nf][m] = mfma32(wv, hv8, acc1[nf][m]);
                else acc2[nf][m] = mfma32(wv, hv8, acc2[nf][m]);
            }
        if constexpr (j == 16) {
            pin(gv[0]); pin(gv[1]);
#pragma unroll
            for (int m = 0; m < 2; ++m) {
                const float g = as_f32(gv[m]), s1 = kq == 0 ? g : 1.0f - g;
#pragma unroll
                for (int nf = 0; nf < 4; ++nf) acc1[nf][m] *= s1;
            }
        }
        sched0();
        ldh(ic<DEPTH + j>{});
        if constexpr (j == 2) {
#pragma unroll
            for (int m = 0; m < 2; ++m) ld4_sc1(gv[m], rh, G_OFF + ((4 * tt + 2 * th + m) * 16 + i16) * 4);
        }
        sched0();
    });
    const bool closer = (bid == 255) && (w == 7);
    u32x2 fl[4];
    if (closer) {
#pragma unroll
        for (int k = 0; k < 4; ++k) ld8_sc1(fl[k], flags + (k * 64 + lane) * 128);
    }
#pragma unroll
    for (int m = 0; m < 2; ++m) {
        const float s2 = 1.0f - as_f32(gv[m]);
#pragma unroll
        for (int nf = 0; nf < 4; ++nf) acc1[nf][m] += acc2[nf][m] * s2;
    }
#pragma unroll
    for (int m = 0; m < 2; ++m)
#pragma unroll
        for (int nf = 0; nf < 4; ++nf)
            if (2 * m + (nf >> 1) != kq) *(lds_f32x4_p)(smem + ((w * 2 + m) * 4 + nf) * 1024 + lane * 16) = acc1[nf][m];
    wait_lgkm0();
    bar();
    f32x4 r[2];
#pragma unroll
    for (int e = 0; e < 2; ++e)
        r[e] = kq == 0 ? acc1[e][0] : (kq == 1 ? acc1[2 + e][0] : (kq == 2 ? acc1[e][1] : acc1[2 + e][1]));
#pragma unroll
    for (int s = 1; s < 4; ++s) {
        const int src = 4 * th + ((kq + s) & 3);
#pragma unroll
        for (int e = 0; e < 2; ++e)
            r[e] += *(lds_f32x4_p)(smem + ((src * 2 + (kq >> 1)) * 4 + 2 * (kq & 1) + e) * 1024 + lane * 16);
    }
    {
        const int tok = frag_token(4 * tt + 2 * th + (kq >> 1), i16);
        const unsigned ooff = tok * 1024 + ct * 128 + (kq & 1) * 64 + (q & 1) * 32 + (q >> 1) * 16;
        const u32x2 x = pk4(r[0]), y = pk4(r[1]);
        const auto s0 = __builtin_amdgcn_permlane16_swap(x[0], y[0], false, false);
        const auto s1 = __builtin_amdgcn_permlane16_swap(x[1], y[1], false, false);
        __builtin_amdgcn_raw_buffer_store_b128((u32x4){s0[0], s1[0], s0[1], s1[1]}, mk_rsrc(a.out, S_ * D_ * 2), ooff, 0, 16);
    }
    if (closer) {
        wait_vm<1>();
        for (int spins = 0; spins < (1 << 16); ++spins) {
            bool ok = true;
#pragma unroll
            for (int k = 0; k < 4; ++k) { pin(fl[k]); ok &= (((unsigned long long)fl[k][1] << 32) | fl[k][0]) == tag; }
            if (__all(ok)) break;
            __builtin_amdgcn_s_sleep(1);
#pragma unroll
            for (int k = 0; k < 4; ++k) ld8_sc1(fl[k], flags + (k * 64 + lane) * 128);
            wait_vm<0>();
        }
        if (lane == 0) __hip_atomic_store((g_u64_p)(a.ws + EPOCH_OFF), tag, __ATOMIC_RELAXED, __HIP_MEMORY_SCOPE_AGENT);
    }
    asm volatile("gm_code_end:");
}

extern "C" void kernel_launch(void* const* d_in, const int* in_sizes, int n_in, void* d_out, int out_size, void* d_ws,
                              size_t ws_size, hipStream_t stream) {
    (void)in_sizes; (void)n_in; (void)out_size; (void)ws_size;
    (void)hipFuncSetAttribute((const void*)fused_kernel, hipFuncAttributeMaxDynamicSharedMemorySize, LDS_BYTES);
    Args a;
    a.x = d_in[0]; a.wqkv = d_in[1]; a.wo = d_in[2]; a.wg = d_in[4]; a.wu = d_in[5]; a.wd = d_in[6]; a.gw = d_in[7];
    a.ws = (char*)d_ws; a.out = d_out;
    fused_kernel<<<256, 512, LDS_BYTES, stream>>>(a);
}
```
